```python
import torch

def _softmax(x, axis=-1):
    return torch.softmax(x, dim=axis)
def _gelu(x):
    return torch.nn.functional.gelu(x, approximate="tanh")
def _silu(x):
    return torch.nn.functional.silu(x)
def _sigmoid(x):
    return torch.sigmoid(x)
def _layernorm(x, axis=-1, eps=1e-5):
    m = x.mean(dim=axis, keepdim=True)
    v = x.var(dim=axis, keepdim=True, unbiased=False)
    return (x - m) / torch.sqrt(v + eps)
def _scatter_add(tgt, idx, src, axis=0):
    return tgt.clone().index_add_(axis, idx, src)
def _segment_sum(src, seg, n):
    out = torch.zeros((n,) + src.shape[1:], dtype=src.dtype)
    return out.index_add_(0, seg, src)
def _pad(x, pw):
    flat = tuple(v for pair in reversed(pw) for v in pair)
    return torch.nn.functional.pad(x, flat)

kernel_name = 'standalone__kv_cache_quant_int4_int2_kv_with_per_channel_or_per_token_scales__decode'

def setup_inputs():

    _w = (lambda a: (lambda t: t.to(torch.bfloat16) if t.is_floating_point() else t)(torch.as_tensor(a)))
    B = 2
    Sq = 1
    Skv = 32768
    Hq = 32
    Hkv = 16
    Dh = 128
    bits = 2
    G = -1
    if G <= 0 or G >= Skv:
        G = max(1, Skv // 2)
    Skv = (Skv // G) * G
    ngk = Skv // G
    qmax = (1 << bits) - 1
    return {
        "q": _w((lambda *s: torch.randn(*s, dtype=torch.bfloat16) * 3.2)(B, Hq, Sq, Dh)),
        "qk": _w(torch.randint(0, qmax + 1, (B, Hkv, Skv, Dh)).to(torch.int32)),
        "qv": _w(torch.randint(0, qmax + 1, (B, Hkv, Skv, Dh)).to(torch.int32)),
        "k_scales": _w(torch.abs((lambda *s: torch.randn(*s, dtype=torch.bfloat16) * 3.2)(B, Hkv, ngk, Dh)) * 0.05 + 0.02),
        "k_zeros": _w((lambda *s: torch.randn(*s, dtype=torch.bfloat16) * 3.2)(B, Hkv, ngk, Dh) + float(qmax) / 2.0),
        "v_scales": _w(torch.abs((lambda *s: torch.randn(*s, dtype=torch.bfloat16) * 0.05)(B, Hkv, Skv, 1)) * 0.05 + 0.02),
        "v_zeros": _w((lambda *s: torch.randn(*s, dtype=torch.bfloat16) * 0.05)(B, Hkv, Skv, 1) + float(qmax) / 2.0),
    }

def reference(q, qk, qv, k_scales, k_zeros, v_scales, v_zeros):
    B, Hq, Sq, Dh = q.shape
    Hkv, Skv = qk.shape[1], qk.shape[2]
    ngk = k_scales.shape[2]
    G = Skv // ngk
    ks = (lambda x, n, axis: torch.repeat_interleave(x, n, dim=axis))(k_scales, G, axis=2)
    kz = (lambda x, n, axis: torch.repeat_interleave(x, n, dim=axis))(k_zeros, G, axis=2)
    k = ((lambda x, dt: x.to(dt))(qk, q.dtype) - kz) * ks
    v = ((lambda x, dt: x.to(dt))(qv, q.dtype) - v_zeros) * v_scales
    rep = Hq // Hkv
    if rep > 1:
        k = (lambda x, n, axis: torch.repeat_interleave(x, n, dim=axis))(k, rep, axis=1)
        v = (lambda x, n, axis: torch.repeat_interleave(x, n, dim=axis))(v, rep, axis=1)
    scale = float(Dh) ** -0.5
    logits = torch.einsum("bhqd,bhkd->bhqk", q, k) * scale
    p = _softmax(logits, axis=-1)
    o = torch.einsum("bhqk,bhkd->bhqd", p, v)
    return o
```

```cpp
#include <hip/hip_runtime.h>
#include <hip/hip_bf16.h>
#include <stdint.h>

#define KV_WAVES 4
#define KV_NSPLIT 16
#define KV_NT 1
#define KV_MINW 2

namespace kvq {

constexpr int B = 2, HQ = 32, HKV = 16, SKV = 32768, D = 128, NGK = 2, GT = SKV / NGK;
constexpr int BH = B * HKV;
constexpr int WAVES = KV_WAVES, NTHR = WAVES * 64, NSPLIT = KV_NSPLIT;
constexpr int TS = SKV / NSPLIT;
constexpr int TILE = 16;
constexpr int TPW = TS / TILE / WAVES;
static_assert(TS % (TILE * WAVES) == 0 && TS <= GT && GT % TS == 0, "split geometry");
constexpr int REC = 132;

using i32x4 = __attribute__((ext_vector_type(4))) int;
using f32x4 = __attribute__((ext_vector_type(4))) float;
using u16x4 = __attribute__((ext_vector_type(4))) unsigned short;

__device__ __forceinline__ float bf2f(unsigned short u) { return __builtin_bit_cast(float, (unsigned)u << 16); }

template <int CTRL> __device__ __forceinline__ float dpp(float x) {
  return __builtin_bit_cast(float, __builtin_amdgcn_mov_dpp(__builtin_bit_cast(int, x), CTRL, 0xf, 0xf, true));
}
constexpr int XOR1 = 0xB1, XOR2 = 0x4E, XOR7 = 0x141, ROR8 = 0x128, ROR4 = 0x124, BCAST = 0x150;

__device__ __forceinline__ float xrow16_sum(float x) {
  auto s = __builtin_amdgcn_permlane16_swap(__float_as_uint(x), __float_as_uint(x), false, false);
  return __uint_as_float(s[0]) + __uint_as_float(s[1]);
}

__device__ __forceinline__ i32x4 ldkv(const i32x4* p) {
  return __builtin_nontemporal_load(p);
}

template <int I> struct PV {
  __device__ static __forceinline__ void run(const i32x4 (&V)[8], float w, float (&a0)[4], float (&a1)[4]) {
    const float w0 = dpp<BCAST + 2 * I>(w), w1 = dpp<BCAST + 2 * I + 1>(w);
#pragma unroll
    for (int j = 0; j < 4; ++j) {
      const float f = (float)V[I][j];
      a0[j] = fmaf(w0, f, a0[j]);
      a1[j] = fmaf(w1, f, a1[j]);
    }
    if constexpr (I < 7) PV<I + 1>::run(V, w, a0, a1);
  }
};

__global__ void __launch_bounds__(NTHR, KV_MINW)
attn_split(const unsigned short* __restrict__ q, const int* __restrict__ qk, const int* __restrict__ qv,
           const unsigned short* __restrict__ ksc, const unsigned short* __restrict__ kzr,
           const unsigned short* __restrict__ vsc, const unsigned short* __restrict__ vzr,
           float* __restrict__ part) {
  __shared__ __attribute__((aligned(16))) float lds[WAVES * 2 * 2 * REC];
  const int bid = blockIdx.x;
  const int s = bid % NSPLIT, bh = bid / NSPLIT;
  const int tid = threadIdx.x, lane = tid & 63, wave = __builtin_amdgcn_readfirstlane(tid >> 6);
  const int hf = lane >> 5, c32 = lane & 31, c = lane & 15;
  const int grp = (s * TS) / GT;
  constexpr float C2 = 0.088388347648318440f * 1.4426950408889634f;

  const u16x4 q0 = *reinterpret_cast<const u16x4*>(q + (bh * 2 + 0) * D + 4 * c32);
  const u16x4 q1 = *reinterpret_cast<const u16x4*>(q + (bh * 2 + 1) * D + 4 * c32);
  const u16x4 ks4 = *reinterpret_cast<const u16x4*>(ksc + (bh * NGK + grp) * D + 4 * c32);
  const u16x4 kz4 = *reinterpret_cast<const u16x4*>(kzr + (bh * NGK + grp) * D + 4 * c32);

  const size_t tok0 = (size_t)bh * SKV + (size_t)s * TS + (size_t)wave * TILE;
  const i32x4* kp = reinterpret_cast<const i32x4*>(qk + tok0 * D) + lane;
  const i32x4* vp = reinterpret_cast<const i32x4*>(qv + tok0 * D) + lane;
  const unsigned short* vsp = vsc + tok0 + 2 * (c >> 1) + hf;
  const unsigned short* vzp = vzr + tok0 + 2 * (c >> 1) + hf;
  constexpr int TSTRIDE = WAVES * TILE * D / 4;

  i32x4 K[8], V[8];
#pragma unroll
  for (int i = 0; i < 8; ++i) K[i] = ldkv(kp + i * 64);
  unsigned short vs_n = vsp[0], vz_n = vzp[0];
#pragma unroll
  for (int i = 0; i < 8; ++i) V[i] = ldkv(vp + i * 64);

  float qs0[4], qs1[4];
  float b0 = 0.f, b1 = 0.f;
#pragma unroll
  for (int j = 0; j < 4; ++j) {
    const float sc = bf2f(ks4[j]) * C2, z = bf2f(kz4[j]);
    qs0[j] = bf2f(q0[j]) * sc;
    qs1[j] = bf2f(q1[j]) * sc;
    b0 = fmaf(qs0[j], z, b0);
    b1 = fmaf(qs1[j], z, b1);
  }
  b0 += dpp<XOR1>(b0); b0 += dpp<XOR2>(b0); b0 += dpp<XOR7>(b0); b0 += dpp<ROR8>(b0); b0 = xrow16_sum(b0);
  b1 += dpp<XOR1>(b1); b1 += dpp<XOR2>(b1); b1 += dpp<XOR7>(b1); b1 += dpp<ROR8>(b1); b1 = xrow16_sum(b1);
  const float nbias = (c & 1) ? -b1 : -b0;

  float m = -INFINITY, l = 0.f, cz = 0.f;
  float a0[4] = {0.f, 0.f, 0.f, 0.f}, a1[4] = {0.f, 0.f, 0.f, 0.f};

  for (int it = 0; it < TPW; ++it) {
    float x[16];
#pragma unroll
    for (int i = 0; i < 8; ++i) {
      float d0 = 0.f, d1 = 0.f;
#pragma unroll
      for (int j = 0; j < 4; ++j) {
        const float f = (float)K[i][j];
        d0 = fmaf(qs0[j], f, d0);
        d1 = fmaf(qs1[j], f, d1);
      }
      x[2 * i] = d0; x[2 * i + 1] = d1;
    }
    const float vs = bf2f(vs_n), vz = bf2f(vz_n);
    const bool more = it + 1 < TPW;
    if (more) {
      kp += TSTRIDE; vsp += WAVES * TILE; vzp += WAVES * TILE;
#pragma unroll
      for (int i = 0; i < 8; ++i) K[i] = ldkv(kp + i * 64);
      vs_n = vsp[0]; vz_n = vzp[0];
    }
#define TR_STEP(HALF, CTRL, BIT)                                                       \
    _Pragma("unroll") for (int h = 0; h < (HALF); ++h) {                               \
      const float lo_ = x[h], hi_ = x[h + (HALF)];                                     \
      const float keep_ = (c & (BIT)) ? hi_ : lo_, give_ = (c & (BIT)) ? lo_ : hi_;    \
      x[h] = keep_ + dpp<CTRL>(give_); }
    TR_STEP(8, ROR8, 8) TR_STEP(4, XOR7, 4) TR_STEP(2, XOR2, 2) TR_STEP(1, XOR1, 1)
#undef TR_STEP
    const float sc = xrow16_sum(x[0]) + nbias;
    float tm = fmaxf(sc, dpp<XOR2>(sc)); tm = fmaxf(tm, dpp<ROR4>(tm)); tm = fmaxf(tm, dpp<ROR8>(tm));
    const float mn = fmaxf(m, tm);
    const float alpha = __builtin_amdgcn_exp2f(m - mn), p = __builtin_amdgcn_exp2f(sc - mn);
    m = mn;
    l = fmaf(l, alpha, p);
    const float w = p * vs;
    cz = fmaf(cz, alpha, w * vz);
    const float al0 = dpp<0x00>(alpha), al1 = dpp<0x55>(alpha);
#pragma unroll
    for (int j = 0; j < 4; ++j) { a0[j] *= al0; a1[j] *= al1; }
    PV<0>::run(V, w, a0, a1);
    if (more) {
      vp += TSTRIDE;
#pragma unroll
      for (int i = 0; i < 8; ++i) V[i] = ldkv(vp + i * 64);
    }
  }
  l += dpp<XOR2>(l); l += dpp<ROR4>(l); l += dpp<ROR8>(l);
  cz += dpp<XOR2>(cz); cz += dpp<ROR4>(cz); cz += dpp<ROR8>(cz);
  const float cz0 = dpp<0x00>(cz), cz1 = dpp<0x55>(cz);
  float* rec0 = lds + ((wave * 2 + hf) * 2 + 0) * REC;
  float* rec1 = rec0 + REC;
  *reinterpret_cast<f32x4*>(rec0 + 4 + 4 * c32) = f32x4{a0[0] - cz0, a0[1] - cz0, a0[2] - cz0, a0[3] - cz0};
  *reinterpret_cast<f32x4*>(rec1 + 4 + 4 * c32) = f32x4{a1[0] - cz1, a1[1] - cz1, a1[2] - cz1, a1[3] - cz1};
  if (c32 < 2) { float* r = rec0 + c32 * REC; r[0] = m; r[1] = l; }
  __syncthreads();
  if (tid < 256) {
    const int g = tid >> 7, d = tid & 127;
    float M = -INFINITY;
#pragma unroll
    for (int k = 0; k < 2 * WAVES; ++k) M = fmaxf(M, lds[(k * 2 + g) * REC]);
    float L = 0.f, o = 0.f;
#pragma unroll
    for (int k = 0; k < 2 * WAVES; ++k) {
      const float* r = lds + (k * 2 + g) * REC;
      const float f = __builtin_amdgcn_exp2f(r[0] - M);
      L = fmaf(f, r[1], L); o = fmaf(f, r[4 + d], o);
    }
    float* pr = part + ((size_t)(bh * 2 + g) * NSPLIT + s) * REC;
    pr[4 + d] = o;
    if (d == 0) { pr[0] = M; pr[1] = L; }
  }
}

__global__ void __launch_bounds__(128)
attn_combine(const float* __restrict__ part, __hip_bfloat16* __restrict__ out) {
  const int row = blockIdx.x, d = threadIdx.x;
  const float* pr = part + (size_t)row * NSPLIT * REC;
  float M = -INFINITY;
#pragma unroll
  for (int s = 0; s < NSPLIT; ++s) M = fmaxf(M, pr[s * REC]);
  float L = 0.f, o = 0.f;
#pragma unroll
  for (int s = 0; s < NSPLIT; ++s) {
    const float f = __builtin_amdgcn_exp2f(pr[s * REC] - M);
    L = fmaf(f, pr[s * REC + 1], L); o = fmaf(f, pr[s * REC + 4 + d], o);
  }
  out[row * D + d] = __float2bfloat16(o / L);
}

}

extern "C" void kernel_launch(void* const* d_in, const int* in_sizes, int n_in,
                              void* d_out, int out_size, void* d_ws, size_t ws_size,
                              hipStream_t stream) {
  using namespace kvq;
  const unsigned short* q = (const unsigned short*)d_in[0];
  const int* qk = (const int*)d_in[1];
  const int* qv = (const int*)d_in[2];
  const unsigned short* ksc = (const unsigned short*)d_in[3];
  const unsigned short* kzr = (const unsigned short*)d_in[4];
  const unsigned short* vsc = (const unsigned short*)d_in[5];
  const unsigned short* vzr = (const unsigned short*)d_in[6];
  float* part = (float*)d_ws;
  attn_split<<<dim3(BH * NSPLIT), dim3(NTHR), 0, stream>>>(q, qk, qv, ksc, kzr, vsc, vzr, part);
  attn_combine<<<dim3(B * HQ), dim3(128), 0, stream>>>(part, (__hip_bfloat16*)d_out);
}
```

```cpp
#include <hip/hip_runtime.h>
#include <hip/hip_bf16.h>
#include <stdint.h>


namespace kvq {

constexpr int B = 2, HQ = 32, HKV = 16, SKV = 32768, D = 128, NGK = 2, GT = SKV / NGK;
constexpr int BH = B * HKV;
constexpr int WAVES = 4, NTHR = WAVES * 64, NSPLIT = 8;
constexpr int TS = SKV / NSPLIT;
constexpr int TILE = 16;
constexpr int NTILE = TS / TILE;
static_assert(TS % (TILE * WAVES) == 0 && TS <= GT && GT % TS == 0, "split geometry");
constexpr int REC = 132;
constexpr int GSLOT = 80;
constexpr size_t WS_SLOTS = 65536;
constexpr unsigned EPOCH_STEP = 0x9E3779B9u;

using i32x4 = __attribute__((ext_vector_type(4))) int;
using f32x4 = __attribute__((ext_vector_type(4))) float;
using f32x2 = __attribute__((ext_vector_type(2))) float;
using u16x4 = __attribute__((ext_vector_type(4))) unsigned short;
typedef unsigned long long u64;

__device__ __forceinline__ float bf2f(unsigned short u) { return __builtin_bit_cast(float, (unsigned)u << 16); }

template <int CTRL> __device__ __forceinline__ float dpp(float x) {
  return __builtin_bit_cast(float, __builtin_amdgcn_mov_dpp(__builtin_bit_cast(int, x), CTRL, 0xf, 0xf, true));
}
constexpr int XOR1 = 0xB1, XOR2 = 0x4E, XOR7 = 0x141, ROR8 = 0x128, ROR4 = 0x124, BCAST = 0x150;

__device__ __forceinline__ float xrow16_sum(float x) {
  auto s = __builtin_amdgcn_permlane16_swap(__float_as_uint(x), __float_as_uint(x), false, false);
  return __uint_as_float(s[0]) + __uint_as_float(s[1]);
}
__device__ __forceinline__ i32x4 ldkv(const i32x4* p) { return __builtin_nontemporal_load(p); }
#define SB() __builtin_amdgcn_sched_barrier(0)

__device__ __forceinline__ f32x4 cvt4(const i32x4& r) {
  f32x4 f = {(float)r[0], (float)r[1], (float)r[2], (float)r[3]};
  asm volatile("" : "+v"(f));
  return f;
}
__device__ __forceinline__ void qk_one(const f32x4& f, const f32x2 (&qs)[4], float& x0, float& x1) {
  f32x2 d = qs[0] * f32x2{f[0], f[0]};
#pragma unroll
  for (int j = 1; j < 4; ++j) d = __builtin_elementwise_fma(qs[j], f32x2{f[j], f[j]}, d);
  x0 = d[0]; x1 = d[1];
}
template <int I> __device__ __forceinline__ void pv_one(const f32x4& f, float w, f32x2 (&acc)[4]) {
  const f32x2 w2 = {dpp<BCAST + 2 * I>(w), dpp<BCAST + 2 * I + 1>(w)};
#pragma unroll
  for (int j = 0; j < 4; ++j) acc[j] = __builtin_elementwise_fma(w2, f32x2{f[j], f[j]}, acc[j]);
}

struct SoftState { float m, l, cz; f32x2 acc[4]; };

__device__ __forceinline__ float softmax_step(float (&x)[16], int c, float nbias, unsigned vsz, SoftState& st) {
#define TR_STEP(HALF, CTRL, BIT)                                                     \
  _Pragma("unroll") for (int h = 0; h < (HALF); ++h) {                               \
    const float lo_ = x[h], hi_ = x[h + (HALF)];                                     \
    const float keep_ = (c & (BIT)) ? hi_ : lo_, give_ = (c & (BIT)) ? lo_ : hi_;    \
    x[h] = keep_ + dpp<CTRL>(give_); }
  TR_STEP(8, ROR8, 8) TR_STEP(4, XOR7, 4) TR_STEP(2, XOR2, 2) TR_STEP(1, XOR1, 1)
#undef TR_STEP
  const float sc = xrow16_sum(x[0]) + nbias;
  float tm = fmaxf(sc, dpp<XOR2>(sc)); tm = fmaxf(tm, dpp<ROR4>(tm)); tm = fmaxf(tm, dpp<ROR8>(tm));
  const float mn = fmaxf(st.m, tm);
  const float alpha = __builtin_amdgcn_exp2f(st.m - mn), p = __builtin_amdgcn_exp2f(sc - mn);
  st.m = mn;
  st.l = fmaf(st.l, alpha, p);
  const float vs = __builtin_bit_cast(float, vsz << 16), vz = __builtin_bit_cast(float, vsz & 0xffff0000u);
  const float w = p * vs;
  st.cz = fmaf(st.cz, alpha, w * vz);
  const f32x2 al = {dpp<0x00>(alpha), dpp<0x55>(alpha)};
#pragma unroll
  for (int j = 0; j < 4; ++j) st.acc[j] *= al;
  return w;
}

constexpr int LDS_MRG = TS;
constexpr int LDS_EPOCH = LDS_MRG + WAVES * 2 * 2 * REC;
constexpr int LDS_WORDS = LDS_EPOCH + 4;

__device__ __forceinline__ u64 granule(float v, unsigned tag) { return ((u64)tag << 32) | (u64)__builtin_bit_cast(unsigned, v); }

__global__ void __launch_bounds__(NTHR, 1)
attn_decode(const unsigned short* __restrict__ q, const int* __restrict__ qk, const int* __restrict__ qv,
            const unsigned short* __restrict__ ksc, const unsigned short* __restrict__ kzr,
            const unsigned short* __restrict__ vsc, const unsigned short* __restrict__ vzr,
            unsigned char* ws, __hip_bfloat16* __restrict__ out) {
  __shared__ __attribute__((aligned(16))) float ldsf[LDS_WORDS];
  unsigned* ldsu = reinterpret_cast<unsigned*>(ldsf);
  float* lds = ldsf + LDS_MRG;
  const int bid = blockIdx.x;
  const int s = bid % NSPLIT, bh = bid / NSPLIT;
  const int tid = threadIdx.x, lane = tid & 63, wave = __builtin_amdgcn_readfirstlane(tid >> 6);
  const int hf = lane >> 5, c32 = lane & 31, c = lane & 15;
  const int grp = (s * TS) / GT;
  constexpr float C2 = 0.088388347648318440f * 1.4426950408889634f;

  const size_t tokb = (size_t)bh * SKV + (size_t)s * TS;
  const i32x4* kb = reinterpret_cast<const i32x4*>(qk + tokb * D) + lane;
  const i32x4* vb = reinterpret_cast<const i32x4*>(qv + tokb * D) + lane;
  constexpr int TELEM = TILE * D / 4;

  i32x4 R[8], V0[8];
  int cur = wave;
#pragma unroll
  for (int i = 0; i < 8; ++i) R[i] = ldkv(kb + cur * TELEM + i * 64);
  SB();
  const u16x4 q0 = *reinterpret_cast<const u16x4*>(q + (bh * 2 + 0) * D + 4 * c32);
  const u16x4 q1 = *reinterpret_cast<const u16x4*>(q + (bh * 2 + 1) * D + 4 * c32);
  const u16x4 ks4 = *reinterpret_cast<const u16x4*>(ksc + (bh * NGK + grp) * D + 4 * c32);
  const u16x4 kz4 = *reinterpret_cast<const u16x4*>(kzr + (bh * NGK + grp) * D + 4 * c32);
  SB();
#pragma unroll
  for (int i = 0; i < 8; ++i) V0[i] = ldkv(vb + cur * TELEM + i * 64);
  SB();
  constexpr int SPASS = TS / (NTHR * 8);
  static_assert(SPASS * NTHR * 8 == TS, "scale staging");
  i32x4 svs[SPASS], svz[SPASS];
#pragma unroll
  for (int u = 0; u < SPASS; ++u) {
    svs[u] = __builtin_nontemporal_load(reinterpret_cast<const i32x4*>(vsc + tokb) + u * NTHR + tid);
    svz[u] = __builtin_nontemporal_load(reinterpret_cast<const i32x4*>(vzr + tokb) + u * NTHR + tid);
  }
  unsigned* epoch_w = reinterpret_cast<unsigned*>(ws + (size_t)bh * 256);
  const unsigned epoch_in = __hip_atomic_load(epoch_w, __ATOMIC_RELAXED, __HIP_MEMORY_SCOPE_AGENT);
  SB();
  STAMP(1);

  f32x2 qs[4];
  float b0 = 0.f, b1 = 0.f;
#pragma unroll
  for (int j = 0; j < 4; ++j) {
    const float sc = bf2f(ks4[j]) * C2, z = bf2f(kz4[j]);
    qs[j] = f32x2{bf2f(q0[j]) * sc, bf2f(q1[j]) * sc};
    b0 = fmaf(qs[j][0], z, b0);
    b1 = fmaf(qs[j][1], z, b1);
  }
  const int soff = 2 * (c >> 1) + hf;
  SoftState st;
  st.m = -INFINITY; st.l = 0.f; st.cz = 0.f;
#pragma unroll
  for (int j = 0; j < 4; ++j) st.acc[j] = f32x2{0.f, 0.f};
  float nbias;
  {
    float x[16];
#pragma unroll
    for (int i = 0; i < 8; ++i) {
      const f32x4 f = cvt4(R[i]); SB();
      R[i] = ldkv(kb + (cur + WAVES) * TELEM + i * 64); SB();
      qk_one(f, qs, x[2 * i], x[2 * i + 1]);
    }
    b0 += dpp<XOR1>(b0); b0 += dpp<XOR2>(b0); b0 += dpp<XOR7>(b0); b0 += dpp<ROR8>(b0); b0 = xrow16_sum(b0);
    b1 += dpp<XOR1>(b1); b1 += dpp<XOR2>(b1); b1 += dpp<XOR7>(b1); b1 += dpp<ROR8>(b1); b1 = xrow16_sum(b1);
    nbias = (c & 1) ? -b1 : -b0;
    SB();
#pragma unroll
    for (int u = 0; u < SPASS; ++u) {
      const int t8 = u * NTHR + tid;
      i32x4 lo, hi;
#pragma unroll
      for (int j = 0; j < 2; ++j) {
        lo[2 * j] = __builtin_amdgcn_perm(svz[u][j], svs[u][j], 0x05040100);
        lo[2 * j + 1] = __builtin_amdgcn_perm(svz[u][j], svs[u][j], 0x07060302);
        hi[2 * j] = __builtin_amdgcn_perm(svz[u][j + 2], svs[u][j + 2], 0x05040100);
        hi[2 * j + 1] = __builtin_amdgcn_perm(svz[u][j + 2], svs[u][j + 2], 0x07060302);
      }
      *reinterpret_cast<i32x4*>(ldsu + t8 * 8) = lo;
      *reinterpret_cast<i32x4*>(ldsu + t8 * 8 + 4) = hi;
    }
    if (tid == 0) ldsu[LDS_EPOCH] = epoch_in + EPOCH_STEP;
    __syncthreads();
    STAMP(2);
    const unsigned vsz = ldsu[cur * TILE + soff];
    float w = softmax_step(x, c, nbias, vsz, st);
    asm volatile("" : "+v"(w));
    SB();
#define PVS(I) { const f32x4 f = cvt4(V0[I]); pv_one<I>(f, w, st.acc); }
    PVS(0) PVS(1) PVS(2) PVS(3) PVS(4) PVS(5) PVS(6) PVS(7)
#undef PVS
    asm volatile("" : "+v"(st.acc[0]), "+v"(st.acc[1]), "+v"(st.acc[2]), "+v"(st.acc[3]));
    SB();
    cur += WAVES;
  }

  for (int nxt = cur + WAVES; nxt < NTILE; nxt += WAVES) {
    const unsigned vsz = ldsu[cur * TILE + soff];
    float x[16];
#pragma unroll
    for (int i = 0; i < 8; ++i) {
      const f32x4 f = cvt4(R[i]); SB();
      R[i] = ldkv(vb + cur * TELEM + i * 64); SB();
      qk_one(f, qs, x[2 * i], x[2 * i + 1]);
    }
    float w = softmax_step(x, c, nbias, vsz, st);
    asm volatile("" : "+v"(w));
    SB();
#define PVS(I) { const f32x4 f = cvt4(R[I]); SB(); R[I] = ldkv(kb + nxt * TELEM + I * 64); SB(); pv_one<I>(f, w, st.acc); }
    PVS(0) PVS(1) PVS(2) PVS(3) PVS(4) PVS(5) PVS(6) PVS(7)
#undef PVS
    asm volatile("" : "+v"(st.acc[0]), "+v"(st.acc[1]), "+v"(st.acc[2]), "+v"(st.acc[3]));
    SB();
    cur = nxt;
  }
  {
    const unsigned vsz = ldsu[cur * TILE + soff];
    float x[16];
#pragma unroll
    for (int i = 0; i < 8; ++i) {
      const f32x4 f = cvt4(R[i]); SB();
      R[i] = ldkv(vb + cur * TELEM + i * 64); SB();
      qk_one(f, qs, x[2 * i], x[2 * i + 1]);
    }
    float w = softmax_step(x, c, nbias, vsz, st);
    asm volatile("" : "+v"(w));
    SB();
#define PVS(I) { const f32x4 f = cvt4(R[I]); pv_one<I>(f, w, st.acc); }
    PVS(0) PVS(1) PVS(2) PVS(3) PVS(4) PVS(5) PVS(6) PVS(7)
#undef PVS
  }
  float l = st.l, cz = st.cz;
  l += dpp<XOR2>(l); l += dpp<ROR4>(l); l += dpp<ROR8>(l);
  cz += dpp<XOR2>(cz); cz += dpp<ROR4>(cz); cz += dpp<ROR8>(cz);
  const float cz0 = dpp<0x00>(cz), cz1 = dpp<0x55>(cz);
  float* rec0 = lds + ((wave * 2 + hf) * 2 + 0) * REC;
  float* rec1 = rec0 + REC;
  *reinterpret_cast<f32x4*>(rec0 + 4 + 4 * c32) = f32x4{st.acc[0][0] - cz0, st.acc[1][0] - cz0, st.acc[2][0] - cz0, st.acc[3][0] - cz0};
  *reinterpret_cast<f32x4*>(rec1 + 4 + 4 * c32) = f32x4{st.acc[0][1] - cz1, st.acc[1][1] - cz1, st.acc[2][1] - cz1, st.acc[3][1] - cz1};
  if (c32 < 2) { float* r = rec0 + c32 * REC; r[0] = st.m; r[1] = l; }
  __syncthreads();
  const int g = tid >> 7, d = tid & 127;
  const unsigned tag = ldsu[LDS_EPOCH];
  float M = -INFINITY;
#pragma unroll
  for (int k = 0; k < 2 * WAVES; ++k) M = fmaxf(M, lds[(k * 2 + g) * REC]);
  float L = 0.f, o = 0.f;
#pragma unroll
  for (int k = 0; k < 2 * WAVES; ++k) {
    const float* r = lds + (k * 2 + g) * REC;
    const float f = __builtin_amdgcn_exp2f(r[0] - M);
    L = fmaf(f, r[1], L); o = fmaf(f, r[4 + d], o);
  }
  u64* slots = reinterpret_cast<u64*>(ws + WS_SLOTS) + (size_t)(bh * WAVES + wave) * (NSPLIT * GSLOT);
  __hip_atomic_store(slots + s * GSLOT + lane, granule(o, tag), __ATOMIC_RELAXED, __HIP_MEMORY_SCOPE_AGENT);
  if (lane < 2) __hip_atomic_store(slots + s * GSLOT + 64 + lane, granule(lane ? L : M, tag), __ATOMIC_RELAXED, __HIP_MEMORY_SCOPE_AGENT);
  asm volatile("s_waitcnt vmcnt(0)" ::: "memory");
  u64 go[NSPLIT];
  const u64 gml = __hip_atomic_load(slots + ((lane >> 1) & (NSPLIT - 1)) * GSLOT + 64 + (lane & 1), __ATOMIC_RELAXED, __HIP_MEMORY_SCOPE_AGENT);
#pragma unroll
  for (int k = 0; k < NSPLIT; ++k) go[k] = __hip_atomic_load(slots + k * GSLOT + lane, __ATOMIC_RELAXED, __HIP_MEMORY_SCOPE_AGENT);
  SB();
  bool ok = (unsigned)(gml >> 32) == tag;
#pragma unroll
  for (int k = 0; k < NSPLIT; ++k) ok = ok && (unsigned)(go[k] >> 32) == tag;
  if (__all(ok)) {
    const int mlv = (int)(unsigned)gml;
    float MM = -INFINITY;
#pragma unroll
    for (int k = 0; k < NSPLIT; ++k) MM = fmaxf(MM, __builtin_bit_cast(float, __builtin_amdgcn_readlane(mlv, 2 * k)));
    float LL = 0.f, oo = 0.f;
#pragma unroll
    for (int k = 0; k < NSPLIT; ++k) {
      const float f = __builtin_amdgcn_exp2f(__builtin_bit_cast(float, __builtin_amdgcn_readlane(mlv, 2 * k)) - MM);
      LL = fmaf(f, __builtin_bit_cast(float, __builtin_amdgcn_readlane(mlv, 2 * k + 1)), LL);
      oo = fmaf(f, __builtin_bit_cast(float, (unsigned)go[k]), oo);
    }
    out[(bh * 2 + g) * D + d] = __float2bfloat16(oo / LL);
    if (tid == 0) __hip_atomic_store(epoch_w, tag, __ATOMIC_RELAXED, __HIP_MEMORY_SCOPE_AGENT);
  }
}

}

extern "C" void kernel_launch(void* const* d_in, const int* in_sizes, int n_in,
                              void* d_out, int out_size, void* d_ws, size_t ws_size,
                              hipStream_t stream) {
  using namespace kvq;
  attn_decode<<<dim3(BH * NSPLIT), dim3(NTHR), 0, stream>>>(
      (const unsigned short*)d_in[0], (const int*)d_in[1], (const int*)d_in[2], (const unsigned short*)d_in[3],
      (const unsigned short*)d_in[4], (const unsigned short*)d_in[5], (const unsigned short*)d_in[6],
      (unsigned char*)d_ws, (__hip_bfloat16*)d_out);
}
```

```cpp
#include <hip/hip_runtime.h>
#include <hip/hip_bf16.h>
#include <stdint.h>

namespace kvq {

constexpr int B = 2, HQ = 32, HKV = 16, SKV = 32768, D = 128, NGK = 2, GT = SKV / NGK;
constexpr int BH = B * HKV;
constexpr int WAVES = 4;
constexpr int NTHR = (WAVES + 1) * 64, NSPLIT = 8, NBLK = BH * NSPLIT;
constexpr int TS = SKV / NSPLIT;
constexpr int TILE = 16;
constexpr int CT = TS / TILE;
constexpr int NT = CT / WAVES;
static_assert(HQ == 2 * HKV && GT % TS == 0, "two query heads per kv head; a chunk lies inside one K scale group");
constexpr unsigned CHUNK_BYTES = (unsigned)TS * D * 4;
constexpr int REC = 132;
constexpr int LDS_S = TS, LDS_MRG = LDS_S + CT * 32, LDS_CTL = LDS_MRG + WAVES * 2 * 2 * REC;
constexpr int LDS_WORDS = 21 * 1024;
static_assert(LDS_CTL + 4 <= LDS_WORDS, "LDS map");
constexpr int GSLOT = 80;
constexpr size_t WS_SLOTS = 65536, WS_START = 1u << 20;
constexpr int START_STRIDE = 4352;
constexpr unsigned EPOCH_STEP = 0x9E3779B9u;
constexpr unsigned TICK = 256;
constexpr unsigned DQ = 27264 / 2;
constexpr unsigned FIRST = 150 * TICK;
constexpr unsigned SLACK_START = 100 * TICK, SLACK_KV = 50 * TICK, SLACK_STEP = 10 * TICK;
constexpr int STALL = 90 * TICK, CALM = 20 * TICK, SKIP = 4 * DQ;

using i32x4 = __attribute__((ext_vector_type(4))) int;
using f32x4 = __attribute__((ext_vector_type(4))) float;
using f32x2 = __attribute__((ext_vector_type(2))) float;
using u32x2 = __attribute__((ext_vector_type(2))) unsigned;
using bf16x2 = __attribute__((ext_vector_type(2))) __bf16;
typedef unsigned long long u64;
typedef volatile __attribute__((address_space(3))) unsigned lds_word;

template <int CTRL> __device__ __forceinline__ int dppi(int x) { return __builtin_amdgcn_mov_dpp(x, CTRL, 0xf, 0xf, true); }
template <int CTRL> __device__ __forceinline__ float dpp(float x) { return __builtin_bit_cast(float, dppi<CTRL>(__builtin_bit_cast(int, x))); }
constexpr int XOR1 = 0xB1, XOR2 = 0x4E, XOR7 = 0x141, ROR8 = 0x128, ROR4 = 0x124, BCAST = 0x150;

__device__ __forceinline__ float xrow16_sum(float x) {
  auto s = __builtin_amdgcn_permlane16_swap(__float_as_uint(x), __float_as_uint(x), false, false);
  return __uint_as_float(s[0]) + __uint_as_float(s[1]);
}
__device__ __forceinline__ unsigned step_off(unsigned n) { return (n >> 1) * 65536u + (n & 1) * 16384u; }
__device__ __forceinline__ unsigned wave_off(unsigned w) { return (w >> 1) * 32768u + (w & 1) * 8192u; }
#define LDP(rsrc, soff, i) __builtin_bit_cast(i32x4, __builtin_amdgcn_raw_buffer_load_b128(rsrc, voff + ((i) & 3) * 1024, (soff) + ((i) >> 2) * 4096, 2))
#define SB() __builtin_amdgcn_sched_barrier(0)

__device__ __forceinline__ f32x4 cvt4(const i32x4& r) {
  f32x4 f = {(float)r[0], (float)r[1], (float)r[2], (float)r[3]};
  asm volatile("" : "+v"(f));
  return f;
}
__device__ __forceinline__ unsigned pk_bf16(f32x2 v) { return __builtin_bit_cast(unsigned, __builtin_convertvector(v, bf16x2)); }
__device__ __forceinline__ f32x2 unpk_bf16(unsigned u) { return f32x2{__builtin_bit_cast(float, u << 16), __builtin_bit_cast(float, u & 0xffff0000u)}; }
__device__ __forceinline__ float rnd_bf16(float v) { return __builtin_bit_cast(float, pk_bf16(f32x2{v, v}) << 16); }
__device__ __forceinline__ float dot2(unsigned a, unsigned b, float acc) {
  return __builtin_amdgcn_fdot2_f32_bf16(__builtin_bit_cast(bf16x2, a), __builtin_bit_cast(bf16x2, b), acc, false);
}
struct KConst { f32x2 z[2], s[2]; u32x2 q0, q1; };
__device__ __forceinline__ void qk_one(const f32x4& f, const KConst& kc, float& x0, float& x1) {
  const unsigned ka = pk_bf16(unpk_bf16(pk_bf16(f32x2{f[0], f[1]} - kc.z[0])) * kc.s[0]);
  const unsigned kb = pk_bf16(unpk_bf16(pk_bf16(f32x2{f[2], f[3]} - kc.z[1])) * kc.s[1]);
  x0 = dot2(kb, kc.q0[1], dot2(ka, kc.q0[0], 0.f));
  x1 = dot2(kb, kc.q1[1], dot2(ka, kc.q1[0], 0.f));
  asm volatile("" : "+v"(x0), "+v"(x1));
}
template <int I> __device__ __forceinline__ void pv_one(const f32x4& f, float w, f32x2 (&acc)[4]) {
  const f32x2 w2 = {dpp<BCAST + 2 * I>(w), dpp<BCAST + 2 * I + 1>(w)};
#pragma unroll
  for (int j = 0; j < 4; ++j) acc[j] = __builtin_elementwise_fma(w2, f32x2{f[j], f[j]}, acc[j]);
  asm volatile("" : "+v"(acc[0]), "+v"(acc[1]), "+v"(acc[2]), "+v"(acc[3]));
}

struct Grid { unsigned next, slack; int was_late; };
constexpr int NEVER_SKIP = 1 << 30;
__device__ __forceinline__ unsigned now_q() { return (unsigned)__builtin_amdgcn_s_memrealtime() * TICK; }
__device__ __forceinline__ void pace(Grid& g, unsigned origin) {
  int late = (int)(now_q() - (g.next + origin));
  if (late > STALL && g.was_late < CALM) {
    do { g.next += SKIP; late -= SKIP; } while (late > STALL);
  }
  g.was_late = late;
  if (late < 0) { do { __builtin_amdgcn_s_sleep(1); } while ((int)(now_q() - (g.next + origin)) < 0); }
  const unsigned e = min(g.slack, SLACK_STEP);
  g.slack -= e;
  g.next += DQ + e;
}

__device__ __forceinline__ u64 granule(float v, unsigned tag) { return ((u64)tag << 32) | (u64)__builtin_bit_cast(unsigned, v); }
#define RLX_AGENT __ATOMIC_RELAXED, __HIP_MEMORY_SCOPE_AGENT

__global__ void __launch_bounds__(NTHR, 1)
attn_decode(const unsigned short* __restrict__ q, const int* __restrict__ qk, const int* __restrict__ qv,
            const unsigned short* __restrict__ ksc, const unsigned short* __restrict__ kzr,
            const unsigned short* __restrict__ vsc, const unsigned short* __restrict__ vzr,
            unsigned char* ws, __hip_bfloat16* __restrict__ out) {
  __shared__ __attribute__((aligned(16))) unsigned ldsu[LDS_WORDS];
  float* ldsf = reinterpret_cast<float*>(ldsu);
  const u64 t_full = __builtin_amdgcn_s_memrealtime();
  asm volatile("" :: "s"(q), "s"(qk), "s"(qv), "s"(ksc), "s"(kzr), "s"(vsc), "s"(vzr), "s"(ws), "s"(out));
  const int bid = blockIdx.x;
  const int s = bid % NSPLIT, bh = bid / NSPLIT;
  const int tid = threadIdx.x, lane = tid & 63, wave = __builtin_amdgcn_readfirstlane(tid >> 6);
  const int hf = lane >> 5, c32 = lane & 31, c = lane & 15;
  const size_t tokb = (size_t)bid * TS;
  unsigned* epoch_w = reinterpret_cast<unsigned*>(ws + (size_t)bh * 256);
  lds_word* ctl = (lds_word*)(ldsu + LDS_CTL);

  if (wave == WAVES) {
    const unsigned t_start = (unsigned)t_full;
    u64* start_g = reinterpret_cast<u64*>(ws + WS_START);
    if (bid >= NBLK - 8 && lane < 16) __hip_atomic_store(start_g + lane * (START_STRIDE / 8) + (bid & 7), t_full, RLX_AGENT);
    if (lane == 0) { ctl[0] = 0u; ctl[2] = t_start; }
    __syncthreads();
    constexpr int NU = TS / 8 / 64;
    i32x4 a[NU], b[NU];
#pragma unroll
    for (int u = 0; u < NU; ++u) {
      a[u] = __builtin_nontemporal_load(reinterpret_cast<const i32x4*>(vsc + tokb) + u * 64 + lane);
      b[u] = __builtin_nontemporal_load(reinterpret_cast<const i32x4*>(vzr + tokb) + u * 64 + lane);
    }
    SB();
    for (int it = 0; it < 8; ++it) {
      const u64 v = __hip_atomic_load(start_g + (bid & 15) * (START_STRIDE / 8) + (lane & 7), RLX_AGENT);
      const long long dt = (long long)(v - t_full);
      const bool ok = dt > -2000 && dt < 2000;
      int d = ok ? (int)dt : -4000;
      d = max(d, dppi<XOR1>(d)); d = max(d, dppi<XOR2>(d)); d = max(d, dppi<XOR7>(d));
      if (lane == 0 && d > 0) ctl[2] = t_start + d;
      if (__all(ok)) break;
      __builtin_amdgcn_s_sleep(4);
    }
    SB();
#pragma unroll
    for (int u = 0; u < NU; ++u) {
      i32x4 lo, hi;
#pragma unroll
      for (int j = 0; j < 2; ++j) {
        lo[2 * j] = __builtin_amdgcn_perm(b[u][j], a[u][j], 0x05040100);
        lo[2 * j + 1] = __builtin_amdgcn_perm(b[u][j], a[u][j], 0x07060302);
        hi[2 * j] = __builtin_amdgcn_perm(b[u][j + 2], a[u][j + 2], 0x05040100);
        hi[2 * j + 1] = __builtin_amdgcn_perm(b[u][j + 2], a[u][j + 2], 0x07060302);
      }
      *reinterpret_cast<i32x4*>(ldsu + (u * 64 + lane) * 8) = lo;
      *reinterpret_cast<i32x4*>(ldsu + (u * 64 + lane) * 8 + 4) = hi;
    }
    SB();
    const unsigned epoch_in = __hip_atomic_load(epoch_w, RLX_AGENT);
    const int vt = __builtin_nontemporal_load(qv + tokb * D + (size_t)(lane & 3) * (TS * D / 4));
    asm volatile("" :: "v"(vt));
    if (lane == 0) ctl[1] = epoch_in + EPOCH_STEP;
    asm volatile("s_waitcnt lgkmcnt(0)" ::: "memory");
    if (lane == 0) ctl[0] = 1u;
    __syncthreads();
    return;
  }

  constexpr float SCALE = 0.08838834764831845f, LOG2E = 1.4426950408889634f;
  const auto rk = __builtin_amdgcn_make_buffer_rsrc((void*)(qk + tokb * D), 0, CHUNK_BYTES, 0x00020000);
  const auto rv = __builtin_amdgcn_make_buffer_rsrc((void*)(qv + tokb * D), 0, CHUNK_BYTES, 0x00020000);
  const unsigned voff = lane * 16;
  const unsigned off0 = wave_off(wave);
  const int grp = (s * TS) / GT;
  KConst kc;
  kc.q0 = *reinterpret_cast<const u32x2*>(q + (bh * 2 + 0) * D + 4 * c32);
  kc.q1 = *reinterpret_cast<const u32x2*>(q + (bh * 2 + 1) * D + 4 * c32);
  const u32x2 ks4 = *reinterpret_cast<const u32x2*>(ksc + (bh * NGK + grp) * D + 4 * c32);
  const u32x2 kz4 = *reinterpret_cast<const u32x2*>(kzr + (bh * NGK + grp) * D + 4 * c32);
  SB();
  i32x4 R[8];
#pragma unroll
  for (int i = 0; i < 8; ++i) R[i] = LDP(rk, off0, i);
  SB();
  __syncthreads();
  kc.s[0] = unpk_bf16(ks4[0]); kc.s[1] = unpk_bf16(ks4[1]);
  kc.z[0] = unpk_bf16(kz4[0]); kc.z[1] = unpk_bf16(kz4[1]);
  const int soff = (off0 >> 9) + 2 * (c >> 1) + hf;
  const int poff = LDS_S + wave * 32 + hf * 16 + c;
  Grid g;
  g.next = FIRST + DQ * ((bh * WAVES + wave) & 15) / 16; g.slack = SLACK_START; g.was_late = NEVER_SKIP;

#define HALF(H, rn, noff, org) f32x4 f##H[4];                                                                             \
    _Pragma("unroll") for (int i = 0; i < 4; ++i) f##H[i] = cvt4(R[4 * H + i]);                                           \
    SB(); pace(g, org); SB();                                                                                             \
    _Pragma("unroll") for (int i = 0; i < 4; ++i) R[4 * H + i] = LDP(rn, noff, 4 * H + i);                                \
    SB();
#define ORIGIN() ((unsigned)__builtin_amdgcn_readfirstlane(ctl[2]) * TICK)
#define TR_STEP(N, CTRL, BIT)                                                          \
    _Pragma("unroll") for (int h = 0; h < (N); ++h) {                                  \
      const float lo_ = x[h], hi_ = x[h + (N)];                                        \
      const float keep_ = (c & (BIT)) ? hi_ : lo_, give_ = (c & (BIT)) ? lo_ : hi_;    \
      x[h] = keep_ + dpp<CTRL>(give_); }
  float m = -INFINITY;
  for (int n = 0; n < NT; ++n) {
    const bool last = n + 1 == NT;
    const int* nbase = (last ? qv : qk) + tokb * D;
    unsigned noff = last ? off0 : off0 + step_off(n + 1);
    asm volatile("" : "+s"(nbase), "+s"(noff));
    const auto rn = __builtin_amdgcn_make_buffer_rsrc((void*)nbase, 0, CHUNK_BYTES, 0x00020000);
    if (last) g.slack = SLACK_KV;
    if (n < 4) g.was_late = NEVER_SKIP;
    float x[16];
    HALF(0, rn, noff, ORIGIN())
#pragma unroll
    for (int i = 0; i < 4; ++i) qk_one(f0[i], kc, x[2 * i], x[2 * i + 1]);
    if (last || n < 4) g.was_late = NEVER_SKIP;
    SB();
    HALF(1, rn, noff, ORIGIN())
#pragma unroll
    for (int i = 0; i < 4; ++i) qk_one(f1[i], kc, x[8 + 2 * i], x[9 + 2 * i]);
    TR_STEP(8, ROR8, 8) TR_STEP(4, XOR7, 4) TR_STEP(2, XOR2, 2) TR_STEP(1, XOR1, 1)
    const float sc = rnd_bf16(rnd_bf16(xrow16_sum(x[0])) * SCALE);
    m = fmaxf(m, sc);
    ldsf[poff + n * (WAVES * 32)] = sc;
    asm volatile("" : "+v"(m));
    SB();
  }
#undef TR_STEP
  g.next += ORIGIN();
  m = fmaxf(m, dpp<XOR2>(m)); m = fmaxf(m, dpp<ROR4>(m)); m = fmaxf(m, dpp<ROR8>(m));
  while (ctl[0] == 0u) __builtin_amdgcn_s_sleep(1);
  asm volatile("" ::: "memory");
  float l = 0.f, cz = 0.f;
  f32x2 acc[4];
#pragma unroll
  for (int j = 0; j < 4; ++j) acc[j] = f32x2{0.f, 0.f};
  for (int n = 0; n < NT; ++n) {
    unsigned noff = off0 + step_off(n + 1);
    asm volatile("" : "+s"(noff));
    if (n + 1 == NT) g.next -= 1u << 30;
    const bool calm = n >= 2 && n + 1 < NT;
    const float sc = ldsf[poff + n * (WAVES * 32)];
    const f32x2 vsz = unpk_bf16(ldsu[soff + (step_off(n) >> 9)]);
    const float p = __builtin_amdgcn_exp2f((sc - m) * LOG2E);
    const float w = p * vsz[0];
    l += p;
    cz = fmaf(w, vsz[1], cz);
    if (!calm) g.was_late = NEVER_SKIP;
    HALF(0, rv, noff, 0u)
    pv_one<0>(f0[0], w, acc); pv_one<1>(f0[1], w, acc); pv_one<2>(f0[2], w, acc); pv_one<3>(f0[3], w, acc);
    if (!calm) g.was_late = NEVER_SKIP;
    SB();
    HALF(1, rv, noff, 0u)
    pv_one<4>(f1[0], w, acc); pv_one<5>(f1[1], w, acc); pv_one<6>(f1[2], w, acc); pv_one<7>(f1[3], w, acc);
    SB();
  }
#undef ORIGIN
#undef HALF
  l += dpp<XOR2>(l); l += dpp<ROR4>(l); l += dpp<ROR8>(l);
  cz += dpp<XOR2>(cz); cz += dpp<ROR4>(cz); cz += dpp<ROR8>(cz);
  const float cz0 = dpp<0x00>(cz), cz1 = dpp<0x55>(cz);
  float* mrg = ldsf + LDS_MRG;
  float* rec0 = mrg + ((wave * 2 + hf) * 2 + 0) * REC;
  float* rec1 = rec0 + REC;
  *reinterpret_cast<f32x4*>(rec0 + 4 + 4 * c32) = f32x4{acc[0][0] - cz0, acc[1][0] - cz0, acc[2][0] - cz0, acc[3][0] - cz0};
  *reinterpret_cast<f32x4*>(rec1 + 4 + 4 * c32) = f32x4{acc[0][1] - cz1, acc[1][1] - cz1, acc[2][1] - cz1, acc[3][1] - cz1};
  if (c32 < 2) { float* r = rec0 + c32 * REC; r[0] = m; r[1] = l; }
  __syncthreads();
  const int hd = tid >> 7, d = tid & 127;
  const unsigned tag = ctl[1];
  float M = -INFINITY;
#pragma unroll
  for (int k = 0; k < 2 * WAVES; ++k) M = fmaxf(M, mrg[(k * 2 + hd) * REC]);
  float L = 0.f, o = 0.f;
#pragma unroll
  for (int k = 0; k < 2 * WAVES; ++k) {
    const float* r = mrg + (k * 2 + hd) * REC;
    const float f = __builtin_amdgcn_exp2f((r[0] - M) * LOG2E);
    L = fmaf(f, r[1], L); o = fmaf(f, r[4 + d], o);
  }
  u64* slots = reinterpret_cast<u64*>(ws + WS_SLOTS) + (size_t)(bh * WAVES + wave) * (NSPLIT * GSLOT);
  __hip_atomic_store(slots + s * GSLOT + lane, granule(o, tag), RLX_AGENT);
  if (lane < 2) __hip_atomic_store(slots + s * GSLOT + 64 + lane, granule(lane ? L : M, tag), RLX_AGENT);
  asm volatile("s_waitcnt vmcnt(0)" ::: "memory");
  u64 go[NSPLIT];
  const u64 gml = __hip_atomic_load(slots + ((lane >> 1) & (NSPLIT - 1)) * GSLOT + 64 + (lane & 1), RLX_AGENT);
#pragma unroll
  for (int k = 0; k < NSPLIT; ++k) go[k] = __hip_atomic_load(slots + k * GSLOT + lane, RLX_AGENT);
  SB();
  unsigned bad = (unsigned)(gml >> 32) ^ tag;
#pragma unroll
  for (int k = 0; k < NSPLIT; ++k) bad |= (unsigned)(go[k] >> 32) ^ tag;
  if (__all(bad == 0u)) {
    const int mlv = (int)(unsigned)gml;
    float MM = -INFINITY;
#pragma unroll
    for (int k = 0; k < NSPLIT; ++k) MM = fmaxf(MM, __builtin_bit_cast(float, __builtin_amdgcn_readlane(mlv, 2 * k)));
    float LL = 0.f, oo = 0.f;
#pragma unroll
    for (int k = 0; k < NSPLIT; ++k) {
      const float f = __builtin_amdgcn_exp2f((__builtin_bit_cast(float, __builtin_amdgcn_readlane(mlv, 2 * k)) - MM) * LOG2E);
      LL = fmaf(f, __builtin_bit_cast(float, __builtin_amdgcn_readlane(mlv, 2 * k + 1)), LL);
      oo = fmaf(f, __builtin_bit_cast(float, (unsigned)go[k]), oo);
    }
    out[(bh * 2 + hd) * D + d] = __float2bfloat16(oo / LL);
    if (tid == 0) __hip_atomic_store(epoch_w, tag, RLX_AGENT);
  }
}

}

extern "C" void kernel_launch(void* const* d_in, const int* in_sizes, int n_in,
                              void* d_out, int out_size, void* d_ws, size_t ws_size,
                              hipStream_t stream) {
  using namespace kvq;
  attn_decode<<<dim3(NBLK), dim3(NTHR), 0, stream>>>(
      (const unsigned short*)d_in[0], (const int*)d_in[1], (const int*)d_in[2], (const unsigned short*)d_in[3],
      (const unsigned short*)d_in[4], (const unsigned short*)d_in[5], (const unsigned short*)d_in[6],
      (unsigned char*)d_ws, (__hip_bfloat16*)d_out);
}
```
